```python
import torch

def _softmax(x, axis=-1):
    return torch.softmax(x, dim=axis)
def _gelu(x):
    return torch.nn.functional.gelu(x, approximate="tanh")
def _silu(x):
    return torch.nn.functional.silu(x)
def _sigmoid(x):
    return torch.sigmoid(x)
def _layernorm(x, axis=-1, eps=1e-5):
    m = x.mean(dim=axis, keepdim=True)
    v = x.var(dim=axis, keepdim=True, unbiased=False)
    return (x - m) / torch.sqrt(v + eps)
def _scatter_add(tgt, idx, src, axis=0):
    return tgt.clone().index_add_(axis, idx, src)
def _segment_sum(src, seg, n):
    out = torch.zeros((n,) + src.shape[1:], dtype=src.dtype)
    return out.index_add_(0, seg, src)
def _pad(x, pw):
    flat = tuple(v for pair in reversed(pw) for v in pair)
    return torch.nn.functional.pad(x, flat)

kernel_name = 'recurrent_scan__gqa_block_cached__dense_activation_squared_relu__decode'

def setup_inputs():

    _w = (lambda a: (lambda t: t.to(torch.bfloat16) if t.is_floating_point() else t)(torch.as_tensor(a)))
    def _A_setup():
        B, Sq, D = 2, 1, 640
        Hq, Hkv, Sk, Dh = 32, 32, 8192, 192
        assert Hq % Hkv == 0, (Hq, Hkv)
        return {
            "x": _w((lambda *s: torch.randn(*s, dtype=torch.bfloat16) * 0.05)(B, Sq, D)),
            "k_cache": _w((lambda *s: torch.randn(*s, dtype=torch.bfloat16) * 1.6)(B, Hkv, Sk, Dh)),
            "v_cache": _w((lambda *s: torch.randn(*s, dtype=torch.bfloat16) * 0.2)(B, Hkv, Sk, Dh)),
            "wq": _w((lambda *s: torch.randn(*s, dtype=torch.bfloat16) * 1.6)(D, Hq * Dh) * (D ** -0.5)),
            "wo": _w((lambda *s: torch.randn(*s, dtype=torch.bfloat16) * 0.2)(Hq * Dh, D) * ((Hq * Dh) ** -0.5)),
        }
    _A_in = _A_setup()

    def _B_setup():
        B, S, D, F = 2, 1, 640, 1536
        return {
            "x": _w((lambda *s: torch.randn(*s, dtype=torch.bfloat16) * 0.05)(B, S, D)),
            "w1": _w((lambda *s: torch.randn(*s, dtype=torch.bfloat16) * 0.1)(D, F)),
            "b1": _w((lambda *s: torch.randn(*s, dtype=torch.bfloat16) * 0.1)(F)),
            "w2": _w((lambda *s: torch.randn(*s, dtype=torch.bfloat16) * 0.1)(F, D)),
            "b2": _w((lambda *s: torch.randn(*s, dtype=torch.bfloat16) * 0.1)(D)),
        }
    _B_in = _B_setup()

    return {
        "A_x": _A_in["x"],
        "A_k_cache": _A_in["k_cache"],
        "A_v_cache": _A_in["v_cache"],
        "A_wq": _A_in["wq"],
        "A_wo": _A_in["wo"],
        "B_x": _B_in["x"],
        "B_w1": _B_in["w1"],
        "B_b1": _B_in["b1"],
        "B_w2": _B_in["w2"],
        "B_b2": _B_in["b2"],
    }

def reference(A_x, A_k_cache, A_v_cache, A_wq, A_wo, B_x, B_w1, B_b1, B_w2, B_b2):
    def _A_ref(x, k_cache, v_cache, wq, wo):
        B, Sq, D = x.shape
        Hq, Dh = 32, 192
        Hkv = k_cache.shape[1]
        rep = Hq // Hkv
        q = (lambda x, *d: x.permute(*d))((x @ wq).reshape(B, Sq, Hq, Dh), 0, 2, 1, 3)
        k = (lambda x, n, axis: torch.repeat_interleave(x, n, dim=axis))(k_cache, rep, axis=1)
        v = (lambda x, n, axis: torch.repeat_interleave(x, n, dim=axis))(v_cache, rep, axis=1)
        scores = torch.einsum("bhqd,bhkd->bhqk", q, k) * (Dh ** -0.5)
        p = _softmax(scores, axis=-1)
        o = torch.einsum("bhqk,bhkd->bhqd", p, v)
        return (lambda x, *d: x.permute(*d))(o, 0, 2, 1, 3).reshape(B, Sq, Hq * Dh) @ wo

    def _B_ref(x, w1, b1, w2, b2):
        h = torch.einsum("bsd,df->bsf", x, w1) + b1
        zero = torch.zeros_like(h)
        r = torch.where(h > zero, h, zero)
        h = r * r
        out = torch.einsum("bsf,fd->bsd", h, w2) + b2
        return out

    x = A_x
    xn = _layernorm(x, axis=-1)
    s = _A_ref(xn, A_k_cache, A_v_cache, A_wq, A_wo)
    g = _silu(_B_ref(xn, B_w1, B_b1, B_w2, B_b2))
    return x + s * g
```

```cpp
#include <hip/hip_runtime.h>
#include <hip/hip_bf16.h>
#include <stdint.h>

typedef unsigned short u16;
typedef unsigned u32x4 __attribute__((ext_vector_type(4)));
typedef float f32x4 __attribute__((ext_vector_type(4)));
typedef __bf16 bf16x2 __attribute__((ext_vector_type(2)));
typedef __bf16 bf16x8 __attribute__((ext_vector_type(8)));

constexpr int B = 2, D = 640, H = 32, SK = 8192, DH = 192, F = 1536, HD = H * DH;
constexpr int NSPLIT = 4, KEYS_WG = SK / NSPLIT;
constexpr int NW = 8;

__device__ __forceinline__ float bf2f(u16 v) { return __uint_as_float((unsigned)v << 16); }
__device__ __forceinline__ float bflo(unsigned v) { return __uint_as_float(v << 16); }
__device__ __forceinline__ float bfhi(unsigned v) { return __uint_as_float(v & 0xffff0000u); }
__device__ __forceinline__ float rbf(float x) { return (float)(__bf16)x; }
__device__ __forceinline__ u16 f2bf(float x) { return __builtin_bit_cast(u16, (__bf16)x); }
__device__ __forceinline__ float dot2(unsigned k, unsigned q, float acc) {
  return __builtin_amdgcn_fdot2_f32_bf16(__builtin_bit_cast(bf16x2, k), __builtin_bit_cast(bf16x2, q), acc, false);
}
template <int CTRL> __device__ __forceinline__ float dpp(float x) {
  return __builtin_bit_cast(float, __builtin_amdgcn_mov_dpp(__builtin_bit_cast(int, x), CTRL, 0xf, 0xf, true));
}
constexpr int XOR1 = 0xB1, XOR2 = 0x4E, XOR7 = 0x141, XOR8 = 0x128;
__device__ __forceinline__ float xrow16_max(float x) {
  auto s = __builtin_amdgcn_permlane16_swap(__float_as_uint(x), __float_as_uint(x), false, false);
  x = fmaxf(__uint_as_float(s[0]), __uint_as_float(s[1]));
  auto t = __builtin_amdgcn_permlane32_swap(__float_as_uint(x), __float_as_uint(x), false, false);
  return fmaxf(__uint_as_float(t[0]), __uint_as_float(t[1]));
}
__device__ __forceinline__ float xrow16_sum(float x) {
  auto s = __builtin_amdgcn_permlane16_swap(__float_as_uint(x), __float_as_uint(x), false, false);
  x = __uint_as_float(s[0]) + __uint_as_float(s[1]);
  auto t = __builtin_amdgcn_permlane32_swap(__float_as_uint(x), __float_as_uint(x), false, false);
  return __uint_as_float(t[0]) + __uint_as_float(t[1]);
}
__device__ __forceinline__ float wave_sum(float x) {
  x += dpp<XOR1>(x); x += dpp<XOR2>(x); x += dpp<XOR7>(x); x += dpp<XOR8>(x); return xrow16_sum(x);
}

__device__ __forceinline__ void layernorm_to_lds(const u16* x, float* xn, int tid) {
  const int wave = tid >> 6, lane = tid & 63;
  if (wave < 2) {
    float v[10]; float s = 0.f;
#pragma unroll
    for (int i = 0; i < 10; ++i) { v[i] = bf2f(x[wave * D + lane + 64 * i]); s += v[i]; }
    const float mean = wave_sum(s) * (1.f / D);
    float ss = 0.f;
#pragma unroll
    for (int i = 0; i < 10; ++i) { const float d = v[i] - mean; ss += d * d; }
    const float var = rbf(wave_sum(ss) * (1.f / D));
    const float mb = rbf(mean);
    const float den = rbf(sqrtf(rbf(var + 1e-5f)));
#pragma unroll
    for (int i = 0; i < 10; ++i) xn[wave * D + lane + 64 * i] = rbf(rbf(v[i] - mb) / den);
  }
}

constexpr size_t WS_Q = 0;
constexpr size_t WS_HH = 65536;
constexpr size_t WS_PART = 131072;
constexpr size_t WS_Y = 524288;
constexpr size_t WS_Z = WS_Y + 32 * 2 * 640 * 4;

__global__ void __launch_bounds__(256) k_prep(const u16* x, const u16* wq, const u16* w1, const u16* b1, unsigned char* ws) {
  __shared__ float xn[2 * D];
  __shared__ float red[2][64][33];
  const int tid = threadIdx.x;
  layernorm_to_lds(x, xn, tid);
  __syncthreads();
  const int cb = blockIdx.x * 32;
  const bool isq = cb < HD;
  const u16* w = isq ? wq + cb : w1 + (cb - HD);
  const int ld = isq ? HD : F;
  const int cc = tid & 3, rg = tid >> 2;
  float a0[8], a1[8];
#pragma unroll
  for (int k = 0; k < 8; ++k) { a0[k] = 0.f; a1[k] = 0.f; }
#pragma unroll
  for (int i = 0; i < 10; ++i) {
    const int r = rg + 64 * i;
    const u32x4 v = *(const u32x4*)(w + (size_t)r * ld + cc * 8);
    const float x0 = xn[r], x1 = xn[D + r];
#pragma unroll
    for (int k = 0; k < 4; ++k) {
      a0[2 * k] += x0 * bflo(v[k]); a0[2 * k + 1] += x0 * bfhi(v[k]);
      a1[2 * k] += x1 * bflo(v[k]); a1[2 * k + 1] += x1 * bfhi(v[k]);
    }
  }
#pragma unroll
  for (int k = 0; k < 8; ++k) { red[0][rg][cc * 8 + k] = a0[k]; red[1][rg][cc * 8 + k] = a1[k]; }
  __syncthreads();
  if (tid < 64) {
    const int b = tid >> 5, c = tid & 31;
    float s = 0.f;
    for (int r = 0; r < 64; ++r) s += red[b][r][c];
    if (isq) ((u16*)(ws + WS_Q))[b * HD + cb + c] = f2bf(s);
    else {
      const int f = cb - HD + c;
      float h = rbf(rbf(s) + bf2f(b1[f]));
      h = h > 0.f ? h : 0.f;
      ((float*)(ws + WS_HH))[b * F + f] = rbf(h * h);
    }
  }
}

constexpr int GPI = 4;
__global__ void __launch_bounds__(NW * 64) k_attn(const u16* kc, const u16* vc, unsigned char* ws) {
  __shared__ float mrg[NW][200];
  const int tid = threadIdx.x, lane = tid & 63, wave = __builtin_amdgcn_readfirstlane(tid >> 6);
  const int c = lane & 15, g = lane >> 4;
  const int bid = blockIdx.x;
  const int h = (bid & 7) * 4 + ((bid >> 3) & 3), j = bid >> 5;
  const int b = j >> 2, sp = j & 3;
  const u16* q = (const u16*)(ws + WS_Q) + b * HD + h * DH;
  const u32x4 qa = *(const u32x4*)(q + 8 * c);
  const u32x4 qb = *(const u32x4*)(q + 128 + 8 * (c & 7));
  const size_t base = ((size_t)(b * H + h) * SK + sp * KEYS_WG) * DH;
  const int off1 = g * DH + 8 * c, off2 = (4 * (c >> 3) + g) * DH + 128 + 8 * (c & 7);
  const u16* kp = kc + base; const u16* vp = vc + base;
  constexpr int NIT = KEYS_WG / (8 * GPI * NW);
  constexpr float C2 = 0.07216878364870322f * 1.4426950408889634f;
  unsigned msk[4];
#pragma unroll
  for (int w = 0; w < 4; ++w) msk[w] = ((c & 7) >> 1) == w ? ((c & 1) ? 0xffff0000u : 0x0000ffffu) : 0u;
  f32x4 acc0 = {0, 0, 0, 0}, acc1 = {0, 0, 0, 0}, acc2 = {0, 0, 0, 0};
  float m = -INFINITY, l = 0.f;
  u32x4 Kn[GPI][3], Vn[GPI][3];
  auto issue = [&](int it) {
#pragma unroll
    for (int u = 0; u < GPI; ++u) {
      const size_t o = (size_t)((it * NW + wave) * GPI + u) * 8 * DH;
      Kn[u][0] = __builtin_nontemporal_load((const u32x4*)(kp + o + off1));
      Kn[u][1] = __builtin_nontemporal_load((const u32x4*)(kp + o + off1 + 4 * DH));
      Kn[u][2] = __builtin_nontemporal_load((const u32x4*)(kp + o + off2));
    }
#pragma unroll
    for (int u = 0; u < GPI; ++u) {
      const size_t o = (size_t)((it * NW + wave) * GPI + u) * 8 * DH;
      Vn[u][0] = __builtin_nontemporal_load((const u32x4*)(vp + o + off1));
      Vn[u][1] = __builtin_nontemporal_load((const u32x4*)(vp + o + off1 + 4 * DH));
      Vn[u][2] = __builtin_nontemporal_load((const u32x4*)(vp + o + off2));
    }
  };
  issue(0);
  for (int it = 0; it < NIT; ++it) {
    u32x4 Kc[GPI][3], Vc[GPI][3];
#pragma unroll
    for (int u = 0; u < GPI; ++u)
#pragma unroll
      for (int t = 0; t < 3; ++t) { Kc[u][t] = Kn[u][t]; Vc[u][t] = Vn[u][t]; }
    if (it + 1 < NIT) issue(it + 1);
    float s[GPI];
#pragma unroll
    for (int u = 0; u < GPI; ++u) {
      float x0 = 0.f, x1 = 0.f;
#pragma unroll
      for (int k = 0; k < 4; ++k) { x0 = dot2(Kc[u][0][k], qa[k], x0); x1 = dot2(Kc[u][1][k], qa[k], x1); }
      const float a = (c & 8) ? x1 : x0, bb = (c & 8) ? x0 : x1;
      float y = a + dpp<XOR8>(bb);
#pragma unroll
      for (int k = 0; k < 4; ++k) y = dot2(Kc[u][2][k], qb[k], y);
      y += dpp<XOR1>(y); y += dpp<XOR2>(y); y += dpp<XOR7>(y);
      s[u] = y * C2;
    }
    float tm = s[0];
#pragma unroll
    for (int u = 1; u < GPI; ++u) tm = fmaxf(tm, s[u]);
    tm = fmaxf(tm, dpp<XOR8>(tm)); tm = xrow16_max(tm);
    if (__builtin_amdgcn_readfirstlane(__float_as_uint(tm > m ? 1.f : 0.f)) != 0) {
      const float al = __builtin_amdgcn_exp2f(m - tm);
      m = tm; l *= al; acc0 *= al; acc1 *= al; acc2 *= al;
    }
#pragma unroll
    for (int u = 0; u < GPI; ++u) {
      const float p = __builtin_amdgcn_exp2f(s[u] - m);
      const bf16x2 pp = {(__bf16)p, (__bf16)p};
      const unsigned pu = __builtin_bit_cast(unsigned, pp);
      l += bflo(pu);
      const u32x4 A = {pu & msk[0], pu & msk[1], pu & msk[2], pu & msk[3]};
      const bf16x8 Af = __builtin_bit_cast(bf16x8, A);
      acc0 = __builtin_amdgcn_mfma_f32_16x16x32_bf16(Af, __builtin_bit_cast(bf16x8, Vc[u][0]), acc0, 0, 0, 0);
      acc1 = __builtin_amdgcn_mfma_f32_16x16x32_bf16(Af, __builtin_bit_cast(bf16x8, Vc[u][1]), acc1, 0, 0, 0);
      acc2 = __builtin_amdgcn_mfma_f32_16x16x32_bf16(Af, __builtin_bit_cast(bf16x8, Vc[u][2]), acc2, 0, 0, 0);
    }
  }
  float lt = l + dpp<XOR8>(l); lt = xrow16_sum(lt);
  f32x4 o1, o2;
#pragma unroll
  for (int k = 0; k < 4; ++k) {
    float v = g < 2 ? acc0[k] : acc1[k];
    auto t = __builtin_amdgcn_permlane32_swap(__float_as_uint(v), __float_as_uint(v), false, false);
    o1[k] = __uint_as_float(t[0]) + __uint_as_float(t[1]);
    float w = ((g >> 1) == (c >> 3)) ? acc2[k] : 0.f;
    w += dpp<XOR8>(w);
    auto t2 = __builtin_amdgcn_permlane32_swap(__float_as_uint(w), __float_as_uint(w), false, false);
    o2[k] = __uint_as_float(t2[0]) + __uint_as_float(t2[1]);
  }
  if (g < 2) {
    *(f32x4*)&mrg[wave][8 * c + 4 * g] = o1;
    if (c < 8) *(f32x4*)&mrg[wave][128 + 8 * c + 4 * g] = o2;
  }
  if (lane == 0) { mrg[wave][192] = m; mrg[wave][193] = lt; }
  __syncthreads();
  if (tid < 194) {
    float M = mrg[0][192];
#pragma unroll
    for (int w = 1; w < NW; ++w) M = fmaxf(M, mrg[w][192]);
    float r = 0.f;
#pragma unroll
    for (int w = 0; w < NW; ++w) r += __builtin_amdgcn_exp2f(mrg[w][192] - M) * mrg[w][tid == 192 ? 0 : tid];
    if (tid == 192) r = M;
    ((float*)(ws + WS_PART))[((b * H + h) * NSPLIT + sp) * 200 + tid] = r;
  }
}

__global__ void __launch_bounds__(256) k_fin1(const u16* wo, const u16* w2, unsigned char* ws) {
  __shared__ float o[2][240];
  __shared__ float red[24][2][2][80];
  const int tid = threadIdx.x, h = blockIdx.x >> 3, j = blockIdx.x & 7;
  for (int i = tid; i < 2 * 192; i += 256) {
    const int b = i / 192, d = i % 192;
    const float* p = (const float*)(ws + WS_PART) + (b * H + h) * NSPLIT * 200;
    float M = fmaxf(fmaxf(p[192], p[392]), fmaxf(p[592], p[792]));
    float L = 0.f, r = 0.f;
    for (int s = 0; s < 4; ++s) { const float w = __builtin_amdgcn_exp2f(p[s * 200 + 192] - M); L += w * p[s * 200 + 193]; r += w * p[s * 200 + d]; }
    o[b][d] = rbf(r / L);
  }
  if (tid < 96) o[tid / 48][192 + tid % 48] = ((const float*)(ws + WS_HH))[(tid / 48) * F + h * 48 + tid % 48];
  __syncthreads();
  if (tid < 240) {
    const int cc = tid % 10, rg = tid / 10;
    float a[2][2][8];
    for (int u = 0; u < 2; ++u) for (int b = 0; b < 2; ++b) for (int k = 0; k < 8; ++k) a[u][b][k] = 0.f;
#pragma unroll
    for (int i = 0; i < 10; ++i) {
      const int r = rg + 24 * i;
      const u16* w = i < 8 ? wo + (size_t)(h * 192 + r) * D : w2 + (size_t)(h * 48 + r - 192) * D;
      const u32x4 v = *(const u32x4*)(w + 80 * j + 8 * cc);
      const int u = i < 8 ? 0 : 1;
#pragma unroll
      for (int b = 0; b < 2; ++b)
#pragma unroll
        for (int k = 0; k < 4; ++k) { a[u][b][2 * k] += o[b][r] * bflo(v[k]); a[u][b][2 * k + 1] += o[b][r] * bfhi(v[k]); }
    }
    for (int u = 0; u < 2; ++u) for (int b = 0; b < 2; ++b) for (int k = 0; k < 8; ++k) red[rg][u][b][8 * cc + k] = a[u][b][k];
  }
  __syncthreads();
  for (int i = tid; i < 320; i += 256) {
    const int u = i / 160, b = (i / 80) & 1, cidx = i % 80;
    float s = 0.f;
    for (int r = 0; r < 24; ++r) s += red[r][u][b][cidx];
    ((float*)(ws + (u ? WS_Z : WS_Y)))[(h * 2 + b) * D + 80 * j + cidx] = s;
  }
}

__global__ void __launch_bounds__(256) k_fin2(const u16* x, const u16* b2, const unsigned char* ws, u16* out) {
  const int i = blockIdx.x * 256 + threadIdx.x;
  const int b = i / D, cidx = i % D;
  float s = 0.f, z = 0.f;
  for (int h = 0; h < 32; ++h) { s += ((const float*)(ws + WS_Y))[(h * 2 + b) * D + cidx]; z += ((const float*)(ws + WS_Z))[(h * 2 + b) * D + cidx]; }
  s = rbf(s);
  z = rbf(rbf(z) + bf2f(b2[cidx]));
  const float gg = rbf(z / (1.f + __expf(-z)));
  out[i] = f2bf(bf2f(x[i]) + rbf(s * gg));
}

extern "C" void kernel_launch(void* const* d_in, const int* in_sizes, int n_in, void* d_out, int out_size, void* d_ws, size_t ws_size, hipStream_t stream) {
  const u16* x = (const u16*)d_in[0]; const u16* kc = (const u16*)d_in[1]; const u16* vc = (const u16*)d_in[2];
  const u16* wq = (const u16*)d_in[3]; const u16* wo = (const u16*)d_in[4];
  const u16* w1 = (const u16*)d_in[6]; const u16* b1 = (const u16*)d_in[7]; const u16* w2 = (const u16*)d_in[8]; const u16* b2 = (const u16*)d_in[9];
  unsigned char* ws = (unsigned char*)d_ws;
  k_prep<<<240, 256, 0, stream>>>(x, wq, w1, b1, ws);
  k_attn<<<256, NW * 64, 0, stream>>>(kc, vc, ws);
  k_fin1<<<256, 256, 0, stream>>>(wo, w2, ws);
  k_fin2<<<5, 256, 0, stream>>>(x, b2, ws, (u16*)d_out);
}
```

```cpp
#include <hip/hip_runtime.h>
#include <hip/hip_bf16.h>
#include <stdint.h>

typedef unsigned short u16;
typedef unsigned long long u64;
typedef unsigned u32x2 __attribute__((ext_vector_type(2)));
typedef unsigned u32x4 __attribute__((ext_vector_type(4)));
typedef float f32x2 __attribute__((ext_vector_type(2)));
typedef float f32x4 __attribute__((ext_vector_type(4)));
typedef __bf16 bf16x2 __attribute__((ext_vector_type(2)));
typedef __bf16 bf16x8 __attribute__((ext_vector_type(8)));
#define LAS __attribute__((address_space(3)))

constexpr int D = 640, H = 32, SK = 8192, DH = 192, F = 1536, HD = H * DH;
constexpr unsigned KV_BYTES = 2u * H * SK * DH * 2u;
constexpr unsigned WG_BYTES = KV_BYTES / 256;

constexpr int WIN = 33;
constexpr int TAU = 5215;
constexpr float TAUMAX = 5900.f;
constexpr int BUCKET0 = 1000;
constexpr int BUCKET1 = 3000;
constexpr int DELAY1 = 60;
constexpr int WIN0 = 45;
constexpr int DELAY0 = 120;
constexpr float M1 = 1.012f;
constexpr float R1 = 0.972f;
constexpr float R2 = 0.985f;
constexpr float SLOWF = 0.96f;
constexpr float LATE = 10.f;
constexpr int NLATE = 6;
constexpr float UP2 = 1.04f;
constexpr float UP3 = 1.02f;
constexpr int D1 = 24;
constexpr int D1L = 16;
constexpr int D2 = 5;
constexpr int NP = 24;
constexpr int PO = 8;
constexpr int PT = PO + 256;
static_assert(PT % NP + 8 <= NP, "tile pieces do not wrap around the ring");
constexpr int SPIN_CAP = 1 << 19;

constexpr int L_CTL = NP * 6144;
constexpr int C_LANDED = 0, C_ISSUED = 4, C_TAU = 8, C_BKT = 12, C_NXT = 16, C_PROJ = 32, C_Q = 36, C_MRG = 40, C_Y = 44, C_OK = 48, C_XN = 52, C_M = 56, C_L = 60;
constexpr int C_STAMP = 64, C_MV = 128, C_LV = 144;
constexpr int L_XN = L_CTL + 160, L_QS = L_XN + 2560, L_HS = L_QS + 384, L_RED = L_HS + 384, L_YR = L_RED + 8192, L_END = L_YR + 4096;
static_assert(L_END <= 163840, "LDS");

constexpr unsigned W_E = 0, W_M = 4096, W_C = 8192, W_G = 32768, W_P = W_G + 4 * 7680 * 4, W_Y = W_P + 256 * 768, W_Z = W_Y + 160 * 1024, W_END = W_Z + 80 * 1024;
constexpr int SC1 = 16, NT = 2;
constexpr int GAUX = SC1 | NT;

__device__ __forceinline__ float bflo(unsigned v) { return __uint_as_float(v << 16); }
__device__ __forceinline__ float bfhi(unsigned v) { return __uint_as_float(v & 0xffff0000u); }
__device__ __forceinline__ float rbf(float x) { return (float)(__bf16)x; }
__device__ __forceinline__ unsigned pk2(float lo, float hi) { const bf16x2 v = {(__bf16)lo, (__bf16)hi}; return __builtin_bit_cast(unsigned, v); }
__device__ __forceinline__ float dot2(unsigned k, unsigned q, float acc) {
  return __builtin_amdgcn_fdot2_f32_bf16(__builtin_bit_cast(bf16x2, k), __builtin_bit_cast(bf16x2, q), acc, false);
}
template <int CTRL> __device__ __forceinline__ float dpp(float x) {
  return __builtin_bit_cast(float, __builtin_amdgcn_mov_dpp(__builtin_bit_cast(int, x), CTRL, 0xf, 0xf, true));
}
template <int CTRL> __device__ __forceinline__ float dpp_add(float x) {
  asm("" : "+v"(x));
  return x + dpp<CTRL>(x);
}
constexpr int XOR1 = 0xB1, XOR2 = 0x4E, XOR7 = 0x141, XOR8 = 0x128;
__device__ __forceinline__ float x16_sum(float x) {
  auto s = __builtin_amdgcn_permlane16_swap(__float_as_uint(x), __float_as_uint(x), false, false);
  return __uint_as_float(s[0]) + __uint_as_float(s[1]);
}
__device__ __forceinline__ float x32_sum(float x) {
  auto s = __builtin_amdgcn_permlane32_swap(__float_as_uint(x), __float_as_uint(x), false, false);
  return __uint_as_float(s[0]) + __uint_as_float(s[1]);
}
__device__ __forceinline__ float x16_max(float x) {
  auto s = __builtin_amdgcn_permlane16_swap(__float_as_uint(x), __float_as_uint(x), false, false);
  return fmaxf(__uint_as_float(s[0]), __uint_as_float(s[1]));
}
__device__ __forceinline__ float x32_max(float x) {
  auto s = __builtin_amdgcn_permlane32_swap(__float_as_uint(x), __float_as_uint(x), false, false);
  return fmaxf(__uint_as_float(s[0]), __uint_as_float(s[1]));
}
__device__ __forceinline__ float half_sum(float x) {
  x += dpp<XOR1>(x); x += dpp<XOR2>(x); x += dpp<XOR7>(x); x += dpp<XOR8>(x); return x16_sum(x);
}
__device__ __forceinline__ f32x4 mfma(u32x4 a, u32x4 b, f32x4 c) {
  return __builtin_amdgcn_mfma_f32_16x16x32_bf16(__builtin_bit_cast(bf16x8, a), __builtin_bit_cast(bf16x8, b), c, 0, 0, 0);
}
__device__ __forceinline__ u32x4 srd(const void* p, unsigned bytes) {
  const u64 a = (u64)p; return u32x4{(unsigned)a, (unsigned)(a >> 32) & 0xffffu, bytes, 0x00020000u};
}
struct Marks { unsigned m[4]; };
__device__ __forceinline__ Marks marks_of(unsigned epoch) { return Marks{{epoch & 255, (epoch + 0x35) & 255, (epoch + 0x9b) & 255, (epoch + 0xe7) & 255}}; }
__device__ __forceinline__ unsigned mark(float x, unsigned m) { return ((__float_as_uint(x) + 0x80u) & 0xffffff00u) | m; }
__device__ __forceinline__ float unmark(unsigned w) { return __uint_as_float(w & 0xffffff00u); }
__device__ __forceinline__ unsigned stale(const u32x4& w, const Marks& k) { return (((w[0] ^ k.m[0]) | (w[1] ^ k.m[1])) | ((w[2] ^ k.m[2]) | (w[3] ^ k.m[3]))) & 255; }
__device__ __forceinline__ void st128(u32x4 v, __amdgpu_buffer_rsrc_t r, unsigned vo, unsigned so) {
  __builtin_amdgcn_raw_buffer_store_b128(v, r, vo, so, SC1);
  asm volatile("s_nop 1" :: "v"(v) : "memory");
}
__device__ __forceinline__ void st128(f32x4 v, const Marks& k, __amdgpu_buffer_rsrc_t r, unsigned vo, unsigned so) {
  unsigned lo = 255;
  asm("" : "+v"(lo));
  u32x4 w;
#pragma unroll
  for (int i = 0; i < 4; ++i) w[i] = (k.m[i] & lo) | ((__float_as_uint(v[i]) + 0x80u) & ~lo);
  st128(w, r, vo, so);
}
#define CBAR() asm volatile("" ::: "memory")
#define LGKM0() asm volatile("s_waitcnt lgkmcnt(0)" ::: "memory")
#define STR2(x) #x
#define STR(x) STR2(x)

#define A_MIN \
  "v_min3_i32 v60, v60, v61, v62\n\t" \
  "v_min_i32 v60, v60, v63\n\t" \
  "s_nop 0\n\t" \
  "v_readfirstlane_b32 %[lim], v60\n\t" \
  "s_nop 1\n\t" \
  "s_add_i32 %[lim], %[lim], %[np]\n\t"
#define A_FREE \
  "s_cmp_lt_i32 %[P], %[lim]\n\t" \
  "s_cbranch_scc1 Lgo_%=\n\t" \
  "s_mov_b32 %[t0], %[cap]\n" \
  "Lsp_%=:\n\t" \
  "ds_read_b128 v[60:63], %[vctl] offset:16\n\t" \
  "s_waitcnt lgkmcnt(0)\n\t" \
  A_MIN \
  "s_cmp_lt_i32 %[P], %[lim]\n\t" \
  "s_cbranch_scc1 Lgo_%=\n\t" \
  "s_sleep 1\n\t" \
  "s_sub_i32 %[t0], %[t0], 1\n\t" \
  "s_cmp_gt_i32 %[t0], 0\n\t" \
  "s_cbranch_scc1 Lsp_%=\n\t" \
  "s_mov_b32 %[cap], 0\n" \
  "Lgo_%=:\n\t"
#define A_W "s_waitcnt vmcnt(%[win])\n\t"
#define A_CNT \
  "s_getreg_b32 %[t0], hwreg(HW_REG_IB_STS)\n\t" \
  "s_bfe_u32 %[t1], %[t0], 0x20016\n\t" \
  "s_and_b32 %[t0], %[t0], 15\n\t" \
  "s_lshl4_add_u32 %[t1], %[t1], %[t0]\n\t"
#define A_PUT(BASE, ADD) \
  "s_add_u32 %[t0], %[t1], " ADD "\n\t" \
  "s_mul_i32 %[t0], %[t0], 43\n\t" \
  "s_lshr_b32 %[t0], %[t0], 8\n\t" \
  "s_sub_i32 %[t0], " BASE ", %[t0]\n\t" \
  "v_mov_b32 v64, %[t0]\n\t" \
  "v_mov_b32 v65, " BASE "\n\t" \
  "ds_write_b64 %[vctl], v[64:65]\n\t"
#define A_LANDED(ISS) A_CNT A_PUT(ISS, "5")

#define A_PACE(X) \
      "s_lshl_b32 %[t0], s90, 8\n\t" \
      "s_sub_i32 %[t1], %[due], %[t0]\n\t" \
      "s_sub_i32 %[t0], %[t0], s91\n\t" \
      "s_add_i32 s91, s91, %[t1]\n\t" \
      "s_cmp_lt_i32 s91, 0\n\t" \
      "s_cselect_b32 %[due], %[t0], %[due]\n\t" \
      "s_add_u32 %[due], %[due], s92\n\t" \
      "s_min_i32 %[t1], %[t1], %[mxd]\n" \
      "Ldl" X "_%=:\n\t" \
      "s_sub_i32 %[t1], %[t1], %[step]\n\t" \
      "s_cmp_gt_i32 %[t1], 0\n\t" \
      "s_cbranch_scc1 Ldl" X "_%=\n\t"

__device__ __forceinline__ void phase_kv(u32x4 r, unsigned va, unsigned vctl, unsigned vst, unsigned so, unsigned dU, int Pend,
                                         unsigned& S, int& P, int& lim, int& cap, unsigned& due) {
  unsigned t0, t1, soU;
  asm volatile(
      "s_nop 1\n\ts_add_u32 %[soU], %[so], %[dU]\n"
      "Ltop_%=:\n\t"
      A_FREE
      "s_mov_b32 m0, %[S]\n\t"
      "s_add_u32 s93, %[S], 3072\n\t"
      "buffer_load_dwordx4 %[va], %[r], %[so] offen nt lds\n\t" A_W
      A_CNT
      "s_mov_b32 m0, s93\n\t"
      "s_nop 1\n\t"
      "buffer_load_dwordx4 %[va], %[r], %[soU] offen nt lds\n\t" A_W
      A_PUT("%[P]", "4")
      "s_mov_b32 m0, %[S]\n\t"
      "s_memrealtime s[90:91]\n\t"
      "buffer_load_dwordx4 %[va], %[r], %[so] offen offset:1024 nt lds\n\t" A_W
      "ds_read_b128 v[60:63], %[vctl] offset:16\n\t"
      "ds_read_b64 v[66:67], %[vctl] offset:8\n\t"
      "s_mov_b32 m0, s93\n\t"
      "s_nop 1\n\t"
      "buffer_load_dwordx4 %[va], %[r], %[soU] offen offset:1024 nt lds\n\t" A_W
      "s_mov_b32 m0, %[S]\n\t"
      "s_add_u32 %[S], %[S], 6144\n\t"
      "s_cmp_eq_u32 %[S], %[ring]\n\t"
      "s_cselect_b32 %[S], 0, %[S]\n\t"
      "s_add_i32 %[P], %[P], 1\n\t"
      "buffer_load_dwordx4 %[va], %[r], %[so] offen offset:2048 nt lds\n\t" A_W
      "s_add_u32 %[so], %[so], 3072\n\t"
      "s_mov_b32 m0, s93\n\t"
      "s_waitcnt lgkmcnt(0)\n\t"
      A_MIN
      "buffer_load_dwordx4 %[va], %[r], %[soU] offen offset:2048 nt lds\n\t" A_W
      "v_readfirstlane_b32 s92, v66\n\t"
      "v_readfirstlane_b32 s91, v67\n\t"
      "s_add_u32 %[soU], %[soU], 3072\n\t"
      A_PACE("")
      "s_cmp_lt_i32 %[P], %[Pend]\n\t"
      "s_cbranch_scc1 Ltop_%=\n\t"
      "v_mov_b32 v64, s90\n\t"
      "ds_write_b32 %[vst], v64"
      : [lim] "+s"(lim), [cap] "+s"(cap), [due] "+s"(due), [S] "+s"(S), [P] "+s"(P), [so] "+s"(so), [t0] "=&s"(t0), [t1] "=&s"(t1), [soU] "=&s"(soU)
      : [r] "s"(r), [va] "v"(va), [vctl] "v"(vctl), [vst] "v"(vst), [dU] "s"(dU), [Pend] "s"(Pend),
        [np] "i"(NP), [mxd] "i"(4 * 5200), [step] "i"(300), [ring] "i"(NP * 6144), [win] "i"(WIN)
      : "memory", "scc", "s90", "s91", "s92", "s93", "v60", "v61", "v62", "v63", "v64", "v65", "v66", "v67");
}
__device__ __forceinline__ void pre_tile(u32x4 r, unsigned vo, unsigned vctl, unsigned so, unsigned step) {
  unsigned t0, t1, S, P;
#define A_PL(k) "s_add_u32 m0, %[S], " STR(k) "*1024\n\ts_nop 0\n\tbuffer_load_dwordx4 %[vo], %[r], %[so] offen nt lds\n\ts_add_u32 %[so], %[so], %[step]\n\t" A_W
  asm volatile(
      "s_nop 2\n\ts_movk_i32 %[S], 6144\n\ts_mov_b32 %[P], 1\n"
      "Lq_%=:\n\t"
      A_PL(0) A_PL(1) A_PL(2) A_PL(3) A_PL(4) A_PL(5)
      "s_add_u32 %[S], %[S], 6144\n\ts_add_u32 %[P], %[P], 1\n\t"
      A_LANDED("%[P]")
      "s_cmp_lt_u32 %[P], %[po]\n\t"
      "s_cbranch_scc1 Lq_%="
      : [so] "+s"(so), [t0] "=&s"(t0), [t1] "=&s"(t1), [S] "=&s"(S), [P] "=&s"(P)
      : [r] "s"(r), [vo] "v"(vo), [vctl] "v"(vctl), [step] "s"(step), [po] "i"(PO), [win] "i"(WIN0)
      : "memory", "scc", "v64", "v65");
}
__device__ __forceinline__ unsigned idle_until(unsigned vctl, int P, unsigned t) {
  unsigned t0, t1, now;
  asm volatile(
      "Li_%=:\n\t"
      "s_memrealtime s[90:91]\n\t"
      A_LANDED("%[P]")
      "s_waitcnt lgkmcnt(0)\n\t"
      "s_sub_i32 %[t0], s90, %[t]\n\t"
      "s_cmp_lt_i32 %[t0], 0\n\t"
      "s_cbranch_scc1 Li_%=\n\t"
      "s_mov_b32 %[now], s90"
      : [t0] "=&s"(t0), [t1] "=&s"(t1), [now] "=&s"(now) : [vctl] "v"(vctl), [P] "s"(P), [t] "s"(t) : "memory", "scc", "s90", "s91", "v64", "v65");
  return now;
}
__device__ __forceinline__ void quad_tile(u32x4 r, unsigned vt, unsigned vctl, const unsigned (&a)[4], unsigned S0, unsigned S1, int P, int& lim, int& cap) {
  unsigned t0, t1;
#define A_TL(S, k, w, i) "s_add_u32 m0, %[" S "], " STR(k) "*1024\n\ts_add_u32 %[t1], %[a" STR(w) "], " STR(i) "*1280\n\tbuffer_load_dwordx4 %[vt], %[r], %[t1] offen nt lds\n\t"
  asm volatile(
      "s_nop 1\n\t"
      A_FREE
      "s_waitcnt vmcnt(57)\n\t"
      A_TL("S0", 0, 0, 0) A_TL("S0", 1, 1, 0) A_TL("S0", 2, 2, 0) A_TL("S0", 3, 3, 0) A_TL("S0", 4, 0, 1) A_TL("S0", 5, 1, 1)
      "s_nop 0\n\t"
      A_LANDED("%[P]")
      "s_waitcnt vmcnt(57)\n\t"
      A_TL("S1", 0, 2, 1) A_TL("S1", 1, 3, 1) A_TL("S1", 2, 0, 2) A_TL("S1", 3, 1, 2) A_TL("S1", 4, 2, 2) A_TL("S1", 5, 3, 2)
      "s_add_i32 s92, %[P], 1\n\t"
      A_LANDED("s92")
      : [lim] "+s"(lim), [cap] "+s"(cap), [t0] "=&s"(t0), [t1] "=&s"(t1)
      : [r] "s"(r), [vt] "v"(vt), [vctl] "v"(vctl), [a0] "s"(a[0]), [a1] "s"(a[1]), [a2] "s"(a[2]), [a3] "s"(a[3]),
        [S0] "s"(S0), [S1] "s"(S1), [P] "s"(P), [np] "i"(NP)
      : "memory", "scc", "s92", "v60", "v61", "v62", "v63", "v64", "v65");
}
__device__ __forceinline__ void drain(unsigned vctl, int P) {
  unsigned t0, t1;
  asm volatile(
      "Ldr_%=:\n\t"
      A_LANDED("%[P]")
      "s_cmp_lg_u32 %[t1], 0\n\t"
      "s_cbranch_scc1 Ldr_%=\n\t"
      "s_waitcnt vmcnt(0) lgkmcnt(0)\n"
      "Lfd_end0:"
      : [t0] "=&s"(t0), [t1] "=&s"(t1) : [vctl] "v"(vctl), [P] "s"(P) : "memory", "scc", "v64", "v65");
}

typedef unsigned u32x8 __attribute__((ext_vector_type(8)));
typedef unsigned u32x16 __attribute__((ext_vector_type(16)));
#define P_LD(a, d, SO, OFF) "buffer_load_dwordx4 v[" #a ":" #d "], %[vo], %[r], %[" SO "] offen offset:" #OFF " sc1\n\t"
#define P_W(N) "v_mov_b32 %[t], 0\n\tv_mov_b32 %[u], 0\n\ts_waitcnt vmcnt(" #N ")\n\t"
#define P_CK(a, b, c, d)   \
  "v_bitop3_b32 %[t], v" #a ", %[m0], %[t] bitop3:0xbe\n\tv_bitop3_b32 %[u], v" #b ", %[m1], %[u] bitop3:0xbe\n\t" \
  "v_bitop3_b32 %[t], v" #c ", %[m2], %[t] bitop3:0xbe\n\tv_bitop3_b32 %[u], v" #d ", %[m3], %[u] bitop3:0xbe\n\t"
#define P_BR(L) "v_bitop3_b32 %[t], %[t], %[u], %[lo] bitop3:0xa8\n\tv_cmp_ne_u32 vcc, 0, %[t]\n\ts_cmp_eq_u64 vcc, 0\n\ts_cbranch_scc1 " L "_%=\n\t"
#define P_LOOP "s_sub_i32 %[cap], %[cap], 16\n\ts_cmp_gt_i32 %[cap], 0\n\ts_cbranch_scc1 Lp_%=\n"
#define P_O(o, a) "v_bfi_b32 %[o" #o "], %[lo], 0, v" #a "\n\t"
#define P_O4(o0, o1, o2, o3, a, b, c, d) P_O(o0, a) P_O(o1, b) P_O(o2, c) P_O(o3, d)
#define P_MARKS [m0] "s"(k.m[0]), [m1] "s"(k.m[1]), [m2] "s"(k.m[2]), [m3] "s"(k.m[3]), [lo] "v"(255u)
#define P_OUT(n) [o##n] "=&v"(o[n])
#define P1_A P_LD(200, 203, "so", 0) P_LD(204, 207, "so", 64) P_LD(208, 211, "so", 128)
#define P1_B P_LD(212, 215, "so", 0) P_LD(216, 219, "so", 64) P_LD(220, 223, "so", 128)
struct Poll1 { u32x8 a0, b0; u32x4 a1, b1; };
__device__ __forceinline__ void poll1_a(Poll1& p, u32x4 r, unsigned vo, unsigned so) {
  asm volatile("s_nop 4\n\t" P1_A : "={v[200:207]}"(p.a0), "={v[208:211]}"(p.a1) : [r] "s"(r), [vo] "v"(vo), [so] "s"(so) : "memory");
}
__device__ __forceinline__ void poll1_b(Poll1& p, u32x4 r, unsigned vo, unsigned so) {
  asm volatile("s_nop 4\n\t" P1_B : "={v[212:219]}"(p.b0), "={v[220:223]}"(p.b1) : [r] "s"(r), [vo] "v"(vo), [so] "s"(so) : "memory");
}
__device__ __forceinline__ void poll1_loop(Poll1& p, u32x4 r, unsigned vo, unsigned so, const Marks& k, int& cap, float (&o)[12]) {
  unsigned t, u;
  asm volatile(
      "s_nop 2\n"
      "Lp_%=:\n\t"
      P_W(3) P_CK(200, 201, 202, 203) P_CK(204, 205, 206, 207) P_CK(208, 209, 210, 211) P_BR("La") P1_A
      P_W(3) P_CK(212, 213, 214, 215) P_CK(216, 217, 218, 219) P_CK(220, 221, 222, 223) P_BR("Lb") P1_B
      P_LOOP
      "La_%=:\n\t"
      P_O4(0, 1, 2, 3, 200, 201, 202, 203) P_O4(4, 5, 6, 7, 204, 205, 206, 207) P_O4(8, 9, 10, 11, 208, 209, 210, 211)
      "s_branch Le_%=\n"
      "Lb_%=:\n\t"
      P_O4(0, 1, 2, 3, 212, 213, 214, 215) P_O4(4, 5, 6, 7, 216, 217, 218, 219) P_O4(8, 9, 10, 11, 220, 221, 222, 223)
      "Le_%=:\n\t"
      "s_nop 1"
      : "+{v[200:207]}"(p.a0), "+{v[208:211]}"(p.a1), "+{v[212:219]}"(p.b0), "+{v[220:223]}"(p.b1), [cap] "+s"(cap), [t] "=&v"(t), [u] "=&v"(u),
        P_OUT(0), P_OUT(1), P_OUT(2), P_OUT(3), P_OUT(4), P_OUT(5), P_OUT(6), P_OUT(7), P_OUT(8), P_OUT(9), P_OUT(10), P_OUT(11)
      : [r] "s"(r), [vo] "v"(vo), [so] "s"(so), P_MARKS
      : "memory", "scc", "vcc");
}
__device__ __forceinline__ void poll1_end(Poll1& p) {
  asm volatile("s_waitcnt vmcnt(0)" :: "{v[200:207]}"(p.a0), "{v[208:211]}"(p.a1), "{v[212:219]}"(p.b0), "{v[220:223]}"(p.b1) : "memory");
}
#define P2_A P_LD(192, 195, "s0", 0) P_LD(196, 199, "s1", 0) P_LD(200, 203, "s2", 0) P_LD(204, 207, "s3", 0)
#define P2_B P_LD(208, 211, "s0", 0) P_LD(212, 215, "s1", 0) P_LD(216, 219, "s2", 0) P_LD(220, 223, "s3", 0)
struct Poll2 { u32x16 a, b; };
__device__ __forceinline__ void poll2(Poll2& p, u32x4 r, unsigned vo, unsigned so, unsigned stride, const Marks& k, int& cap, float (&o)[16]) {
  unsigned t, u;
  asm volatile(
      "s_nop 4\n\t" P2_A "s_sleep 3\n\t" P2_B
      "Lp_%=:\n\t"
      P_W(4) P_CK(192, 193, 194, 195) P_CK(196, 197, 198, 199) P_CK(200, 201, 202, 203) P_CK(204, 205, 206, 207) P_BR("La") P2_A
      P_W(4) P_CK(208, 209, 210, 211) P_CK(212, 213, 214, 215) P_CK(216, 217, 218, 219) P_CK(220, 221, 222, 223) P_BR("Lb") P2_B
      P_LOOP
      "La_%=:\n\t"
      P_O4(0, 1, 2, 3, 192, 193, 194, 195) P_O4(4, 5, 6, 7, 196, 197, 198, 199) P_O4(8, 9, 10, 11, 200, 201, 202, 203) P_O4(12, 13, 14, 15, 204, 205, 206, 207)
      "s_branch Le_%=\n"
      "Lb_%=:\n\t"
      P_O4(0, 1, 2, 3, 208, 209, 210, 211) P_O4(4, 5, 6, 7, 212, 213, 214, 215) P_O4(8, 9, 10, 11, 216, 217, 218, 219) P_O4(12, 13, 14, 15, 220, 221, 222, 223)
      "Le_%=:\n\t"
      "s_nop 1"
      : "=&{v[192:207]}"(p.a), "=&{v[208:223]}"(p.b), [cap] "+s"(cap), [t] "=&v"(t), [u] "=&v"(u),
        P_OUT(0), P_OUT(1), P_OUT(2), P_OUT(3), P_OUT(4), P_OUT(5), P_OUT(6), P_OUT(7),
        P_OUT(8), P_OUT(9), P_OUT(10), P_OUT(11), P_OUT(12), P_OUT(13), P_OUT(14), P_OUT(15)
      : [r] "s"(r), [vo] "v"(vo), [s0] "s"(so), [s1] "s"(so + stride), [s2] "s"(so + 2 * stride), [s3] "s"(so + 3 * stride), P_MARKS
      : "memory", "scc", "vcc");
}
__device__ __forceinline__ void poll2_end(Poll2& p) {
  asm volatile("s_waitcnt vmcnt(0)\nLfd_end1:" :: "{v[192:207]}"(p.a), "{v[208:223]}"(p.b) : "memory");
}

__global__ void __launch_bounds__(320, 1)
fused_decode(const u16* __restrict__ x, const u16* __restrict__ kc, const u16* __restrict__ vc_, const u16* __restrict__ wq, const u16* __restrict__ wo,
             const u16* __restrict__ w1, const u16* __restrict__ b1, const u16* __restrict__ w2, const u16* __restrict__ b2,
             unsigned char* ws, u16* out) {
  __shared__ __attribute__((aligned(16))) unsigned char smem[L_END];
  LAS unsigned char* L = (LAS unsigned char*)smem;
  const int tid = threadIdx.x, lane = tid & 63;
  unsigned pf = 0;
  {
    u64 pc; unsigned len, len1;
    asm volatile("s_getpc_b64 %0\nLfd_start:\n\ts_mov_b32 %1, Lfd_end0-Lfd_start\n\ts_mov_b32 %2, Lfd_end1-Lfd_start\n\ts_max_i32 %1, %1, %2" : "=s"(pc), "=&s"(len), "=&s"(len1) :: "scc");
    const unsigned o = min((blockIdx.x >> 3) * 1024u + lane * 128u, len - 4) & ~3u;
    u64 ex;
    asm volatile("v_cmp_gt_u32 vcc, 8, %3\n\ts_and_saveexec_b64 %1, vcc\n\ts_cbranch_execz Lfd_skip\n\tglobal_load_dword %0, %2, %4\nLfd_skip:\n\ts_mov_b64 exec, %1"
                 : "+v"(pf), "=&s"(ex) : "v"(o), "v"(tid - 64), "s"(pc) : "memory", "vcc", "scc");
  }
  {
    int v = (tid & 12) == 4 ? ((blockIdx.x >> 3) < 30 ? 1 : PO + tid - 4) : 0;
    asm("" : "+v"(v)); v = tid == 2 ? TAU : v;
    asm("" : "+v"(v)); v = tid == 3 ? BUCKET0 : v;
    *(LAS int*)(L + (tid < 16 ? L_CTL : L_YR) + 4 * tid) = v;
  }
  const int wave = __builtin_amdgcn_readfirstlane(tid >> 6), cw = wave - 1;
  const int bid = blockIdx.x, xcd = bid & 7, wi = bid >> 3, wg = xcd * 32 + wi;
  const int c = lane & 15, g = lane >> 4;
  const bool has_proj = wi < 30, has_wo = wi < 20, has_w2 = wi >= 20 && wi < 30, is_fin = wi >= 27;
  const int tile = xcd * (has_wo ? 20 : 10) + wi - (has_wo ? 0 : 20);
  const int pt = xcd * 30 + wi, cb = pt >> 1, half = pt & 1;
  const bool isq = cb < 96;
  unsigned ld = isq ? HD * 2 : F * 2;
  unsigned pvo = (4 * (c >> 3) + g) * ld + 16 * (c & 7), pso = 320 * half * ld + (isq ? cb : cb - 96) * 128;
  asm volatile("" : "+v"(pvo));
  u32x2 xv[5];
  {
    const unsigned xo = ((lane >> 5) * 160 + (lane & 31)) * 8;
    asm volatile("s_cmp_lg_u32 %7, 1\n\ts_cbranch_scc1 Lfd_nox\n\t"
                 "global_load_dwordx2 %0, %5, %6\n\tglobal_load_dwordx2 %1, %5, %6 offset:256\n\tglobal_load_dwordx2 %2, %5, %6 offset:512\n\t"
                 "global_load_dwordx2 %3, %5, %6 offset:768\n\tglobal_load_dwordx2 %4, %5, %6 offset:1024\nLfd_nox:"
                 : "=&v"(xv[0]), "=&v"(xv[1]), "=&v"(xv[2]), "=&v"(xv[3]), "=&v"(xv[4]) : "v"(xo), "s"(x), "s"(wave) : "memory", "scc");
  }
  asm volatile("s_waitcnt lgkmcnt(0)\n\ts_barrier" : "+s"(ld), "+s"(pso)
               : "s"(x), "s"(kc), "s"(vc_), "s"(wq), "s"(wo), "s"(w1), "s"(b1), "s"(w2), "s"(b2), "s"(ws), "s"(out) : "memory");

  if (wave == 0) {
    const unsigned vctl = (unsigned)(size_t)(L + L_CTL);
    __builtin_amdgcn_s_setprio(3);
    const unsigned tstart = (unsigned)__builtin_amdgcn_s_memrealtime();
    if (has_proj) pre_tile(srd(isq ? wq : w1, 320 * (half + 1) * ld), pvo, vctl, pso, 8 * ld);
    const u32x4 kr = srd(kc, KV_BYTES), vr = srd(vc_, KV_BYTES);
    int lim = NP + 1, cap = SPIN_CAP, P = PO;
    unsigned S = PO * 6144, seg = wg * WG_BYTES;
    u32x4 tr = has_wo ? srd(wo, HD * D * 2) : srd(w2, F * D * 2);
    unsigned vt = g * (has_wo ? 12 : 6) * 1280 + 16 * c;
    unsigned ta[4];
#pragma unroll
    for (int w = 0; w < 4; ++w) ta[w] = ((tile / 5) * 4 + w) * (has_wo ? 48 : 24) * 1280 + (tile % 5) * 256;
    int nlate = has_wo ? 4 : has_w2 ? 2 : 0;
    asm volatile("" : "+s"(tr), "+s"(ta[0]), "+s"(ta[1]), "+s"(ta[2]), "+s"(ta[3]), "+s"(nlate));
    asm volatile("" : "+v"(vt));
    const unsigned now = idle_until(vctl, PO, tstart + (has_proj ? DELAY0 : DELAY1));
    unsigned due = (now << 8) + TAU;
    if (lane == 0) *(volatile LAS unsigned*)(L + L_CTL + C_STAMP) = now;
    auto tile_quad = [&]() {
      const unsigned S1 = S + 6144 == NP * 6144 ? 0 : S + 6144;
      quad_tile(tr, vt, vctl, ta, S, S1, P + 1, lim, cap);
      P += 2; S = S1 + 6144 == NP * 6144 ? 0 : S1 + 6144;
#pragma unroll
      for (int w = 0; w < 4; ++w) ta[w] += 3 * 1280;
    };
#pragma unroll 1
    for (int ph = 0; ph < 12; ++ph) {
      const int G = ((ph >> 1) == 1 || (ph >> 1) == 4) ? 22 : 21;
      phase_kv((ph & 1) ? vr : kr, lane * 16, vctl, vctl + C_STAMP + 4 + 4 * ph, seg, G * 3072, P + G, S, P, lim, cap, due);
      if (ph & 1) seg += 2 * G * 3072;
    }
#pragma unroll 1
    for (; nlate > 0; --nlate) tile_quad();
    drain(vctl, P);
    return;
  }

  const __amdgpu_buffer_rsrc_t wsr = __builtin_amdgcn_make_buffer_rsrc((void*)ws, 0, W_END, 0x00020000);
  volatile LAS int* ctl = (volatile LAS int*)(L + L_CTL);
  unsigned msk[4];
#pragma unroll
  for (int w = 0; w < 4; ++w) msk[w] = ((c & 7) >> 1) == w ? ((c & 1) ? 0xffff0000u : 0x0000ffffu) : 0u;
  int cap = SPIN_CAP;
  auto diag = [&](unsigned pp) { return u32x4{pp & msk[0], pp & msk[1], pp & msk[2], pp & msk[3]}; };
  auto arrive = [&](int off) {
    int old, t;
    asm volatile("s_mov_b64 exec, 1\n\tds_add_rtn_u32 %1, %2, %3\n\ts_mov_b64 exec, -1\n\ts_waitcnt lgkmcnt(0)\n\tv_readfirstlane_b32 %0, %1\n\ts_nop 1"
                 : "=s"(old), "=&v"(t) : "v"(L_CTL + off), "v"(1) : "memory");
    return old;
  };
  auto wait_ge = [&](int off, int n) {
    while (__builtin_amdgcn_readfirstlane(ctl[off / 4]) < n && cap > 0) --cap;
    CBAR();
  };

  unsigned epoch;
  asm volatile("s_load_dword %0, %1, 0x0\n\ts_waitcnt lgkmcnt(0)\n\ts_add_u32 %0, %0, 1" : "=&s"(epoch) : "s"(ws + W_E) : "memory", "scc");
  const unsigned yslot = (has_wo ? W_Y : W_Z) + tile * 1024;
  if (cw == 0) {
    asm volatile("s_waitcnt vmcnt(0)" : "+v"(xv[0]), "+v"(xv[1]), "+v"(xv[2]), "+v"(xv[3]), "+v"(xv[4]), "+v"(pf) :: "memory");
    {
      float v[20];
#pragma unroll
      for (int k = 0; k < 10; ++k) { v[2 * k] = bflo(xv[k >> 1][k & 1]); v[2 * k + 1] = bfhi(xv[k >> 1][k & 1]); }
      float s = 0.f;
#pragma unroll
      for (int k = 0; k < 20; ++k) s += v[k];
      const float mean = half_sum(s) * (1.f / D);
      float ss = 0.f;
#pragma unroll
      for (int k = 0; k < 20; ++k) { const float d0 = v[k] - mean; ss += d0 * d0; }
      const float var = rbf(half_sum(ss) * (1.f / D));
      const float mb = rbf(mean), rd = 1.f / rbf(sqrtf(rbf(var + 1e-5f)));
#pragma unroll
      for (int k = 0; k < 5; ++k)
        ((LAS u32x2*)(L + L_XN))[(lane >> 5) * 160 + (lane & 31) + 32 * k] =
            u32x2{pk2(rbf(v[4 * k] - mb) * rd, rbf(v[4 * k + 1] - mb) * rd), pk2(rbf(v[4 * k + 2] - mb) * rd, rbf(v[4 * k + 3] - mb) * rd)};
    }
    LGKM0();
    if (lane == 0) *(volatile LAS int*)(L + L_CTL + C_XN) = 1;
  }
  int seen = 0;
  auto wait_landed = [&](int P) {
    while (seen <= P) {
      seen = __builtin_amdgcn_readfirstlane(ctl[C_LANDED / 4]);
      if (seen > P) break;
      if (--cap <= 0) { seen = 0x7fffffff; break; }
      if (P < PT - 12) __builtin_amdgcn_s_sleep(1);
    }
    CBAR();
  };
  if (has_proj) {
    wait_ge(C_XN, 1);
    wait_landed(1 + (10 * cw + 9) / 6);
    u32x4 wv[10];
#pragma unroll
    for (int i = 0; i < 10; ++i) wv[i] = *(const LAS u32x4*)(L + 6144 + (10 * cw + i) * 1024 + lane * 16);
    LGKM0();
    if (lane == 0) ctl[C_NXT / 4 + cw] = PO + cw;
#pragma unroll
    for (int i = 0; i < 10; ++i) asm volatile("" : "+v"(wv[i]) :: "memory");
    f32x4 pa[2] = {{0, 0, 0, 0}, {0, 0, 0, 0}};
#pragma unroll
    for (int i = 0; i < 10; ++i)
#pragma unroll
      for (int b = 0; b < 2; ++b) {
        const unsigned v = ((LAS u16*)(L + L_XN))[b * D + 320 * half + 80 * cw + 8 * i + 4 * (c >> 3) + g];
        pa[b] = mfma(diag(v | (v << 16)), wv[i], pa[b]);
      }
    if ((g >> 1) == (c >> 3)) {
#pragma unroll
      for (int b = 0; b < 2; ++b) *(LAS f32x4*)(L + L_RED + (((cw * 2 + (c >> 3)) * 2 + b) * 64 + 8 * (c & 7) + 4 * (g & 1)) * 4) = pa[b];
    }
    if (arrive(C_PROJ) == 3) {
      f32x2 s = {0.f, 0.f};
#pragma unroll
      for (int k = 0; k < 8; ++k) s += *(LAS f32x2*)(L + L_RED + (k * 128 + 2 * lane) * 4);
      const Marks k = marks_of(epoch);
      __builtin_amdgcn_raw_buffer_store_b64(u32x2{mark(s[0], (lane & 1) ? k.m[2] : k.m[0]), mark(s[1], (lane & 1) ? k.m[3] : k.m[1])}, wsr,
                                            (((lane >> 5) * 2 + half) * 7680 + cb * 64 + 2 * (lane & 31)) * 4, W_G, GAUX);
    }
  }
  const Marks mk = marks_of(epoch);
  const unsigned mlane = (lane & 2) ? ((lane & 1) ? mk.m[3] : mk.m[2]) : ((lane & 1) ? mk.m[1] : mk.m[0]);

  constexpr float C2 = 0.07216878364870322f * 1.4426950408889634f;
  u32x4 qa = {0, 0, 0, 0}, qb = {0, 0, 0, 0};
  f32x4 acc0 = {0, 0, 0, 0}, acc1 = {0, 0, 0, 0}, acc2 = {0, 0, 0, 0};
  float m = -INFINITY, l = 0.f;
  float tstar = 1e9f, tau_i = TAU, tau_a = TAU;
  const unsigned po = ((c >> 3) * 32 + tile / 5) * 4 + (c & 3);
  float fw = 0.f;
  int mo0, mo1;
  {
    const int e = lane < 48 ? lane : 0, dq = (e / 12) * 12 + (e & 3) * 3 + (e % 12) / 4, hi = dq >> 5;
    mo0 = L_RED + hi * 1024 + (((dq & 31) >> 1) + 16 * (dq & 1)) * 16;
    mo1 = mo0 + 512 + hi * 128;
  }
  unsigned pv = po * 768 + (12 * cw + g) * 16, yv = ((lane >> 5) * 32 + 2 * c + (g & 1)) * 16;
  asm volatile("" : "+v"(mo0), "+v"(mo1), "+v"(pv), "+v"(yv));
  bool have_ml = false;
  auto ml_ok = [&](const u32x4& e) { return (bool)__all(stale(e, mk) == 0); };
  auto weight = [&](const u32x4& e) {
    const float ms = __uint_as_float((e[0] & 0xffffff00u) | ((e[1] >> 8) & 255));
    float M = fmaxf(ms, dpp<XOR1>(ms)); M = fmaxf(M, dpp<XOR2>(M));
    const float w = __builtin_amdgcn_exp2f(ms - M);
    float Ls = w * unmark(e[2]); Ls += dpp<XOR1>(Ls); Ls += dpp<XOR2>(Ls);
    return w / Ls;
  };
  auto fetch = [&](int P, int next, u32x4 (&d)[6]) {
    wait_landed(P);
    const LAS unsigned char* sp = L + (P % NP) * 6144;
#pragma unroll
    for (int i = 0; i < 6; ++i)
      d[i] = *(const LAS u32x4*)(sp + (i / 3) * 3072 + (i % 3 == 2 ? (4 * (c >> 3) + g) * 384 + 256 + 16 * (c & 7) : g * 384 + 16 * c + (i % 3) * 1536));
    LGKM0();
    if (lane == 0) ctl[C_NXT / 4 + cw] = next;
    asm volatile("" : "+v"(d[0]), "+v"(d[1]), "+v"(d[2]), "+v"(d[3]), "+v"(d[4]), "+v"(d[5]) :: "memory");
  };
  auto score = [&](const u32x4& k0, const u32x4& k1, const u32x4& k2) {
    float x0 = 0.f, x1 = 0.f;
#pragma unroll
    for (int k = 0; k < 4; ++k) { x0 = dot2(k0[k], qa[k], x0); x1 = dot2(k1[k], qa[k], x1); }
    const float a = (c & 8) ? x1 : x0, b = (c & 8) ? x0 : x1;
    float y = a + dpp<XOR8>(b);
#pragma unroll
    for (int k = 0; k < 4; ++k) y = dot2(k2[k], qb[k], y);
    y += dpp<XOR1>(y); y += dpp<XOR2>(y); y += dpp<XOR7>(y);
    return y * C2;
  };
  int Pb = PO, kb = 0;
#pragma unroll 1
  for (int r = 0; r < 6; ++r) {
    const int G = (r == 1 || r == 4) ? 22 : 21;
    const int k0 = (cw - kb) & 3, np = (G - k0 + 3) >> 2;
    const int nk0 = (cw - kb - G) & 3;
    const bool ctrl = cw == 1 && r >= 1 && r <= 4;
    u32x4 kd[6][6];
    const unsigned qo = ((wg >> 7) * 2 * 7680 + ((wg >> 2) & 31) * DH + 64 * cw + lane) * 4;
    unsigned g0, g1;
    u32x2 ce;
#pragma unroll
    for (int j = 0; j < 6; ++j) {
      if (j == 4) {
        const bool lq = r == 0 && cw < 3;
        g0 = __builtin_amdgcn_raw_buffer_load_b32(wsr, lq ? qo + W_G : W_END, 0, GAUX);
        g1 = __builtin_amdgcn_raw_buffer_load_b32(wsr, lq ? qo + W_G : W_END, 7680 * 4, GAUX);
        ce = __builtin_amdgcn_raw_buffer_load_b64(wsr, ctrl ? ((wg & 3) * 64 + lane) * 8 + W_C + r * 2048 : W_END, 0, SC1);
      }
      if (j < np) {
        const int P = Pb + k0 + 4 * j;
        fetch(P, j + 1 < np ? P + 4 : Pb + G + k0, kd[j]);
      }
      if (j == 0 && ctrl && (wg & 3) == 0) {
        const unsigned ta = ctl[C_STAMP / 4 + (r == 1 ? 1 : 2 * r - 2)], tb = ctl[C_STAMP / 4 + 2 * r];
        const float late = (float)((tb - (unsigned)ctl[C_STAMP / 4]) & 0xffffu) + (float)(PT - Pb) * tau_i * (1.f / 256.f) - tstar;
        if (lane < 4)
          __builtin_amdgcn_raw_buffer_store_b64(u32x2{(ta & 0xffffu) | (tb << 16), (epoch << 8) | (unsigned)fminf(fmaxf(late * 0.1f, 0.f), 255.f)}, wsr,
                                                (lane * 64 + (wg >> 2)) * 8, W_C + r * 2048, SC1);
      }
    }
    asm volatile("" : "+v"(g0), "+v"(g1));
    asm volatile("" : "+v"(ce));
    if (r == 0) {
      if (cw < 3) {
        while (!__all((((g0 ^ mlane) | (g1 ^ mlane)) & 255) == 0) && cap > 0) {
          cap -= 8; CBAR();
          g0 = __builtin_amdgcn_raw_buffer_load_b32(wsr, qo + W_G, 0, GAUX);
          g1 = __builtin_amdgcn_raw_buffer_load_b32(wsr, qo + W_G, 7680 * 4, GAUX);
        }
        ((LAS u16*)(L + L_QS))[64 * cw + lane] = (u16)pk2(unmark(g0) + unmark(g1), 0.f);
        arrive(C_Q);
      }
      wait_ge(C_Q, 3);
      qa = *(LAS u32x4*)(L + L_QS + 16 * c); qb = *(LAS u32x4*)(L + L_QS + 256 + 16 * (c & 7));
    }
    if (ctrl) {
      const unsigned now = (unsigned)__builtin_amdgcn_s_memrealtime();
      const int iss = ctl[C_ISSUED / 4];
      const bool ok = (ce[1] >> 8) == (epoch & 0xffffffu);
      const float dab = (float)(((ce[0] >> 16) - ce[0]) & 0xffffu), age = (float)((now - (ce[0] >> 16)) & 0xffffu);
      const float rate = ok ? (float)(r == 1 ? 21 : r == 2 ? 44 : 42) * __builtin_amdgcn_rcpf(dab) : 0.f, late = ok ? (float)(ce[1] & 255) : 0.f;
      const float n = (float)__popcll(__ballot(ok)), rn = __builtin_amdgcn_rcpf(n);
      const float av = x32_sum(half_sum(rate)) * rn, al = x32_sum(half_sum(late)) * rn;
      const float ar = av * (1.f - (64.f - n) * 0.001f);
      const float am = x32_sum(half_sum((float)(PT - Pb) + 0.7f - rate * age)) * (1.f / 64.f);
      const int nslow = __popcll(__ballot(rate < SLOWF * av)), nlate = __popcll(__ballot(!ok || late > al + LATE));
      const bool on = !((ar * tau_a < (r == 1 ? R1 : R2) * 256.f && nslow >= NLATE) || (r > 1 && nlate >= NLATE));
      const float ta = on ? tau_a : fminf(fminf(fmaxf(tau_a * 1.008f, M1 * 256.f * __builtin_amdgcn_rcpf(ar)), tau_a * (r == 1 ? 1.10f : r == 2 ? UP2 : UP3)), TAUMAX);
      const float tnow = (float)((now - (unsigned)ctl[C_STAMP / 4]) & 0xffffu), tn = tnow + am * ta * (1.f / 256.f);
      if (n >= 32.f && iss < PT - 6) {
        tstar = tn; tau_a = ta;
        tau_i = fminf(fmaxf((tn - tnow) * 256.f * __builtin_amdgcn_rcpf((PT - 0.6f) - (float)iss), 4600.f), 7000.f);
        if (lane == 0) { ctl[C_TAU / 4] = (int)tau_i; ctl[C_BKT / 4] = BUCKET1; }
      }
    }
    float s[12];
#pragma unroll
    for (int j = 0; j < 6; ++j) {
      s[2 * j] = score(kd[j][0], kd[j][1], kd[j][2]); s[2 * j + 1] = score(kd[j][3], kd[j][4], kd[j][5]);
    }
    if (np < 6) { s[10] = -INFINITY; s[11] = -INFINITY; }
    float tm = m;
#pragma unroll
    for (int j = 0; j < 12; ++j) tm = fmaxf(tm, s[j]);
    tm = fmaxf(tm, dpp<XOR8>(tm)); tm = x32_max(x16_max(tm));
    if (r == 5) {
      if (lane == 0) *(volatile LAS float*)(L + L_CTL + C_MV + 4 * cw) = tm;
      arrive(C_M); wait_ge(C_M, 4);
      const f32x4 mv = *(const LAS f32x4*)(L + L_CTL + C_MV);
      tm = fmaxf(fmaxf(mv[0], mv[1]), fmaxf(mv[2], mv[3]));
    }
    const float al = __builtin_amdgcn_exp2f(m - tm);
    m = tm; l *= al; acc0 *= al; acc1 *= al; acc2 *= al;
    unsigned pk[12];
#pragma unroll
    for (int j = 0; j < 12; ++j) { const float p = __builtin_amdgcn_exp2f(s[j] - m); l += p; pk[j] = pk2(p, p); }
    if (r == 5) {
      const float lt = x32_sum(x16_sum(l + dpp<XOR8>(l)));
      if (lane == 0) *(volatile LAS float*)(L + L_CTL + C_LV + 4 * cw) = lt;
      if (arrive(C_L) == 3) {
        const f32x4 lv = *(const LAS f32x4*)(L + L_CTL + C_LV);
        if (lane == 0) st128(u32x4{(__float_as_uint(m) & 0xffffff00u) | mk.m[0], (__float_as_uint(m) << 8) | mk.m[1], mark((lv[0] + lv[1]) + (lv[2] + lv[3]), mk.m[2]), mk.m[3]}, wsr, wg * 16, W_M);
      }
    }
    u32x4 ml;
#pragma unroll
    for (int j = 0; j < 6; ++j) {
      if (j == 5) {
        asm volatile("" : "+v"(ml));
        if (r == 5 && has_wo) { have_ml = ml_ok(ml); fw = weight(ml); }
      }
      if (j == 3) {
        ml = __builtin_amdgcn_raw_buffer_load_b128(wsr, (r == 5 && has_wo) ? po * 16 + W_M : W_END, 0, SC1);
      }
      if (j < np) {
        u32x4 d[6];
        const int P = Pb + G + k0 + 4 * j;
        fetch(P, j + 1 < np ? P + 4 : (r < 5 ? Pb + 2 * G + nk0 : 0x3fffffff), d);
#pragma unroll
        for (int u = 0; u < 2; ++u) {
          const u32x4 A = diag(pk[2 * j + u]);
          acc0 = mfma(A, d[3 * u], acc0); acc1 = mfma(A, d[3 * u + 1], acc1); acc2 = mfma(A, d[3 * u + 2], acc2);
        }
      }
    }
    Pb += 2 * G; kb += G;
    if (r == 4 && cw == 2) {
      u64 pc; unsigned len, t;
      asm volatile("s_getpc_b64 %0\nLfd_mid:\n\ts_mov_b32 %1, Lfd_end1-Lfd_mid-4\n\ts_max_i32 %1, %1, Lfd_end0-Lfd_mid-4\n\tv_min_u32 %2, %1, %3\n\tglobal_load_dword %2, %2, %0\n\ts_waitcnt vmcnt(0)"
                   : "=&s"(pc), "=&s"(len), "=&v"(t) : "v"((wi * 4 + (lane & 3)) * 128u) : "memory", "scc");
    }
    if (r == 1 && cw == 3 && has_w2) {
      const int rb = tile / 5;
      unsigned h0[3], h1[3];
      float bias[3];
#pragma unroll
      for (int k = 0; k < 3; ++k) bias[k] = bflo(b1[rb * 96 + (lane + 64 * k) % 96]);
      for (;;) {
        unsigned bad = 0;
#pragma unroll
        for (int k = 0; k < 3; ++k) {
          const unsigned o0 = (((lane + 64 * k) / 96) * 2 * 7680 + HD + rb * 96 + (lane + 64 * k) % 96) * 4;
          h0[k] = __builtin_amdgcn_raw_buffer_load_b32(wsr, o0, W_G, GAUX);
          h1[k] = __builtin_amdgcn_raw_buffer_load_b32(wsr, o0, W_G + 7680 * 4, GAUX);
          bad |= (h0[k] ^ mlane) | (h1[k] ^ mlane);
        }
        if (__all((bad & 255) == 0) || cap <= 0) break;
        cap -= 8; CBAR();
      }
#pragma unroll
      for (int k = 0; k < 3; ++k) {
        float hv = rbf(rbf(unmark(h0[k]) + unmark(h1[k])) + bias[k]);
        hv = hv > 0.f ? hv : 0.f;
        ((LAS u16*)(L + L_HS))[lane + 64 * k] = (u16)pk2(hv * hv, 0.f);
      }
      LGKM0();
      if (lane == 0) ctl[C_OK / 4] = 1;
    }
  }
  {
    bool last;
    {
      f32x4 a;
#pragma unroll
      for (int k = 0; k < 4; ++k) a[k] = g < 2 ? acc0[k] : acc1[k];
      *(LAS f32x4*)(L + L_RED + cw * 2048 + lane * 16) = a;
      *(LAS f32x4*)(L + L_RED + cw * 2048 + 1024 + lane * 16) = acc2;
      last = arrive(C_MRG) == 3;
      if (last) {
        f32x4 t[8];
#pragma unroll
        for (int w = 0; w < 4; ++w) { t[2 * w] = *(const LAS f32x4*)(L + mo0 + w * 2048); t[2 * w + 1] = *(const LAS f32x4*)(L + mo1 + w * 2048); }
        __builtin_amdgcn_sched_barrier(0);
        const f32x4 o = ((t[0] + t[1]) + (t[2] + t[3])) + ((t[4] + t[5]) + (t[6] + t[7]));
        if (lane < 48) st128(o, mk, wsr, lane * 16, W_P + wg * 768);
      }
    }
    auto reduce_y = [&](f32x4 y) {
      *(LAS f32x4*)(L + L_YR + cw * 1024 + lane * 16) = y;
      if (arrive(C_Y) == 3) {
        const LAS f32x4* yp = (const LAS f32x4*)(L + L_YR + lane * 16);
        const f32x4 t = (yp[0] + yp[64]) + (yp[128] + yp[192]);
        st128(t, mk, wsr, yv, yslot);
      }
    };
    if (has_proj) {
      f32x4 y = {0, 0, 0, 0};
      if (has_wo) {
        if (!have_ml) {
          u32x4 e;
          do { cap -= 8; CBAR(); e = __builtin_amdgcn_raw_buffer_load_b128(wsr, po * 16, W_M, SC1); } while (!ml_ok(e) && cap > 0);
          fw = weight(e);
        }
        const u32x4 wsd = srd(ws, W_END);
        Poll1 pl;
        if (last) __builtin_amdgcn_s_sleep(D1L);
        else { wait_ge(C_MRG, 4); __builtin_amdgcn_s_sleep(D1); }
        poll1_a(pl, wsd, pv, W_P);
        __builtin_amdgcn_s_sleep(D2);
        poll1_b(pl, wsd, pv, W_P);
        wait_landed(PT + 7);
        u32x4 wt[12];
#pragma unroll
        for (int i = 0; i < 12; ++i) wt[i] = *(const LAS u32x4*)(L + (PT % NP) * 6144 + (4 * i + cw) * 1024 + lane * 16);
        float ov[12];
        poll1_loop(pl, wsd, pv, W_P, mk, cap, ov);
#pragma unroll
        for (int i = 0; i < 12; ++i) ov[i] *= fw;
        __builtin_amdgcn_sched_barrier(0);
#pragma unroll
        for (int i = 0; i < 12; ++i) ov[i] = dpp_add<XOR1>(ov[i]);
        __builtin_amdgcn_sched_barrier(0);
#pragma unroll
        for (int i = 0; i < 12; ++i) ov[i] = dpp_add<XOR2>(ov[i]);
        __builtin_amdgcn_sched_barrier(0);
        unsigned op[12];
#pragma unroll
        for (int i = 0; i < 12; ++i) op[i] = pk2(ov[i], ov[i]);
        __builtin_amdgcn_sched_barrier(0);
        u32x4 A = diag(op[0]);
#pragma unroll
        for (int i = 0; i < 12; ++i) {
          const u32x4 An = diag(op[i < 11 ? i + 1 : i]);
          __builtin_amdgcn_sched_barrier(0);
          y = mfma(A, wt[i], y);
          __builtin_amdgcn_sched_barrier(0);
          A = An;
        }
        reduce_y(y);
        poll1_end(pl);
      } else {
        wait_ge(C_OK, 1);
        wait_landed(PT + 3);
#pragma unroll
        for (int i = 0; i < 6; ++i) {
          const u32x4 wt = *(const LAS u32x4*)(L + (PT % NP) * 6144 + (4 * i + cw) * 1024 + lane * 16);
          const unsigned v = ((LAS u16*)(L + L_HS))[(c >> 3) * 96 + 24 * cw + 6 * g + i];
          y = mfma(diag(v | (v << 16)), wt, y);
        }
        reduce_y(y);
      }
    }
    if (bid == 248 && cw == 1) {
      for (;;) {
        unsigned bad = 0;
#pragma unroll
        for (int k = 0; k < 4; ++k) {
          bad |= stale(__builtin_amdgcn_raw_buffer_load_b128(wsr, lane * 16, W_M + k * 1024, SC1), mk);
        }
        if (__all(bad == 0) || cap <= 0) break;
        cap -= 8; CBAR();
      }
      if (lane == 0) __builtin_amdgcn_raw_buffer_store_b32(epoch, wsr, W_E, 0, SC1);
    }
    if (is_fin && cw == 0) {
      const int v = xcd * 5 + wi - 27, cs = v >> 3, b = (v >> 2) & 1, q4 = v & 3;
      const int sg = lane >> 3, e = lane & 7;
      const int col = cs * 128 + q4 * 32 + 4 * e + ((lane >> 3) & 1) * 2 + ((lane >> 4) & 1);
      const float xr = bflo(x[b * D + col]), br = bflo(b2[col]);
      const unsigned o = (sg * 5 + cs) * 1024 + (b * 32 + q4 * 8 + e) * 16;
      auto fold = [&](f32x4 t) {
        const bool h3 = lane & 8;
        const float u0 = (h3 ? t[2] : t[0]) + dpp<XOR8>(h3 ? t[0] : t[2]), u1 = (h3 ? t[3] : t[1]) + dpp<XOR8>(h3 ? t[1] : t[3]);
        auto sw = __builtin_amdgcn_permlane16_swap(__float_as_uint(u0), __float_as_uint(u1), false, false);
        return x32_sum(__uint_as_float(sw[0]) + __uint_as_float(sw[1]));
      };
      u32x4 z0, z1;
      for (;;) {
        z0 = __builtin_amdgcn_raw_buffer_load_b128(wsr, o, W_Z, SC1);
        z1 = __builtin_amdgcn_raw_buffer_load_b128(wsr, o, W_Z + 40960, SC1);
        if (__all((stale(z0, mk) | stale(z1, mk)) == 0) || cap <= 0) break;
        cap -= 8; CBAR();
      }
      const float z = rbf(rbf(fold(f32x4{unmark(z0[0]) + unmark(z1[0]), unmark(z0[1]) + unmark(z1[1]), unmark(z0[2]) + unmark(z1[2]), unmark(z0[3]) + unmark(z1[3])})) + br);
      float gt = rbf(z / (1.f + __expf(-z))), xres = xr;
      u16* op = out + b * D + col;
      asm volatile("" : "+v"(gt), "+v"(xres), "+v"(op));
      float ys[16];
      Poll2 pl;
      poll2(pl, srd(ws, W_END), o, W_Y, 40960, mk, cap, ys);
      const float sy = fold(f32x4{(ys[0] + ys[4]) + (ys[8] + ys[12]), (ys[1] + ys[5]) + (ys[9] + ys[13]), (ys[2] + ys[6]) + (ys[10] + ys[14]), (ys[3] + ys[7]) + (ys[11] + ys[15])});
      const unsigned res = pk2(xres + rbf(rbf(sy) * gt), 0.f);
      if (lane < 32) *(__attribute__((address_space(1))) u16*)op = (u16)res;
      poll2_end(pl);
    }
  }
}

extern "C" void kernel_launch(void* const* d_in, const int* in_sizes, int n_in, void* d_out, int out_size, void* d_ws, size_t ws_size, hipStream_t stream) {
  fused_decode<<<256, 320, 0, stream>>>((const u16*)d_in[0], (const u16*)d_in[1], (const u16*)d_in[2], (const u16*)d_in[3], (const u16*)d_in[4],
                                        (const u16*)d_in[6], (const u16*)d_in[7], (const u16*)d_in[8], (const u16*)d_in[9],
                                        (unsigned char*)d_ws, (u16*)d_out);
}
```
